```python
import torch

def _softmax(x, axis=-1):
    return torch.softmax(x, dim=axis)
def _gelu(x):
    return torch.nn.functional.gelu(x, approximate="tanh")
def _silu(x):
    return torch.nn.functional.silu(x)
def _sigmoid(x):
    return torch.sigmoid(x)
def _layernorm(x, axis=-1, eps=1e-5):
    m = x.mean(dim=axis, keepdim=True)
    v = x.var(dim=axis, keepdim=True, unbiased=False)
    return (x - m) / torch.sqrt(v + eps)
def _scatter_add(tgt, idx, src, axis=0):
    return tgt.clone().index_add_(axis, idx, src)
def _segment_sum(src, seg, n):
    out = torch.zeros((n,) + src.shape[1:], dtype=src.dtype)
    return out.index_add_(0, seg, src)
def _pad(x, pw):
    flat = tuple(v for pair in reversed(pw) for v in pair)
    return torch.nn.functional.pad(x, flat)

kernel_name = 'parallel_sum__sliding_block__glu_gated_reglu__prompt'

def setup_inputs():

    _w = (lambda a: (lambda t: t.to(torch.bfloat16) if t.is_floating_point() else t)(torch.as_tensor(a)))
    def _A_setup():
        B, S, D = 1, 2048, 1024
        return {
            "x": _w((lambda *s: torch.randn(*s, dtype=torch.bfloat16) * 0.05)(B, S, D)),
            "wqkv": _w((lambda *s: torch.randn(*s, dtype=torch.bfloat16) * 0.05)(D, 3 * D)),
            "wo": _w((lambda *s: torch.randn(*s, dtype=torch.bfloat16) * 0.05)(D, D)),
        }
    _A_in = _A_setup()

    def _B_setup():
        B, S, D, F = 1, 2048, 1024, 3584
        return {
            "x": _w((lambda *s: torch.randn(*s, dtype=torch.bfloat16) * 0.05)(B, S, D)),
            "W_gate": _w((lambda *s: torch.randn(*s, dtype=torch.bfloat16) * 0.8)(D, F) * (2.0 / D) ** 0.5),
            "W_up": _w((lambda *s: torch.randn(*s, dtype=torch.bfloat16) * 0.8)(D, F) * (2.0 / D) ** 0.5),
            "W_down": _w((lambda *s: torch.randn(*s, dtype=torch.bfloat16) * 0.8)(F, D) * (1.0 / F) ** 0.5),
        }
    _B_in = _B_setup()

    return {
        "A_x": _A_in["x"],
        "A_wqkv": _A_in["wqkv"],
        "A_wo": _A_in["wo"],
        "B_x": _B_in["x"],
        "B_W_gate": _B_in["W_gate"],
        "B_W_up": _B_in["W_up"],
        "B_W_down": _B_in["W_down"],
    }

def reference(A_x, A_wqkv, A_wo, B_x, B_W_gate, B_W_up, B_W_down):
    def _A_ref(x, wqkv, wo):
        B, S, D = x.shape
        H = 32; Dh = D // H; W = 256
        qkv = (lambda x, *d: x.permute(*d))((x @ wqkv).reshape(B, S, 3, H, Dh), 2, 0, 3, 1, 4)
        q, k, v = qkv[0], qkv[1], qkv[2]
        scores = torch.einsum("bhqd,bhkd->bhqk", q, k) * (Dh ** -0.5)
        idx = torch.arange(S)
        mask = (idx[:, None] - idx[None, :] >= 0) & (idx[:, None] - idx[None, :] < W)
        scores = torch.where(mask[None, None], scores, -1e9)
        p = _softmax(scores, axis=-1)
        o = torch.einsum("bhqk,bhkd->bhqd", p, v)
        return (lambda x, *d: x.permute(*d))(o, 0, 2, 1, 3).reshape(B, S, D) @ wo

    def _B_ref(x, W_gate, W_up, W_down):
        gate = torch.einsum("bsd,df->bsf", x, W_gate)
        up = torch.einsum("bsd,df->bsf", x, W_up)
        gate = gate * (lambda x, dt: x.to(dt))(gate > 0, gate.dtype)
        h = gate * up
        return torch.einsum("bsf,fd->bsd", h, W_down)

    x = A_x
    xn = _layernorm(x, axis=-1)
    return x + _A_ref(xn, A_wqkv, A_wo) + _B_ref(xn, B_W_gate, B_W_up, B_W_down)
```

```cpp
#include <hip/hip_runtime.h>
#include <hip/hip_bf16.h>

typedef __attribute__((ext_vector_type(8))) short bf16x8;
typedef __attribute__((ext_vector_type(4))) short bf16x4;
typedef __attribute__((ext_vector_type(4))) float f32x4;
typedef __attribute__((ext_vector_type(4))) unsigned u32x4;
typedef __attribute__((ext_vector_type(2))) unsigned u32x2;
typedef unsigned short u16;
#define LDS_AS __attribute__((address_space(3)))

constexpr int S_ = 2048, D_ = 1024, F_ = 3584, H_ = 32, DH = 32, WIN = 256;

__device__ __forceinline__ float bf2f(u16 v) { return __uint_as_float(((unsigned)v) << 16); }
__device__ __forceinline__ u16 f2bf(float f) {
  unsigned u = __float_as_uint(f);
  u += 0x7fffu + ((u >> 16) & 1u);
  return (u16)(u >> 16);
}
__device__ __forceinline__ float rbf(float f) { return bf2f(f2bf(f)); }
__device__ __forceinline__ unsigned pack2(float a, float b) { return (unsigned)f2bf(a) | ((unsigned)f2bf(b) << 16); }

__device__ __forceinline__ void glds16(const void* g, void* l) {
  __builtin_amdgcn_global_load_lds((const unsigned*)g, (LDS_AS unsigned*)l, 16, 0, 0);
}
__device__ __forceinline__ bf16x4 tr_read(const void* l) {
  return __builtin_amdgcn_ds_read_tr16_b64_v4i16((LDS_AS bf16x4*)l);
}
__device__ __forceinline__ bf16x8 cat8(bf16x4 a, bf16x4 b) {
  return __builtin_shufflevector(a, b, 0, 1, 2, 3, 4, 5, 6, 7);
}

__global__ __launch_bounds__(256) void k_ln(const u16* __restrict__ x, u16* __restrict__ xn, int* __restrict__ cnt) {
  const int row = blockIdx.x * 4 + (threadIdx.x >> 6), lane = threadIdx.x & 63;
  if (blockIdx.x == 0 && threadIdx.x < 128) cnt[threadIdx.x] = 0;
  const u32x4* p = (const u32x4*)(x + (size_t)row * D_);
  u32x4 a = p[lane], b = p[lane + 64];
  float v[16];
#pragma unroll
  for (int i = 0; i < 4; ++i) {
    v[2 * i] = __uint_as_float(a[i] << 16); v[2 * i + 1] = __uint_as_float(a[i] & 0xffff0000u);
    v[8 + 2 * i] = __uint_as_float(b[i] << 16); v[8 + 2 * i + 1] = __uint_as_float(b[i] & 0xffff0000u);
  }
  float s = 0.f;
#pragma unroll
  for (int i = 0; i < 16; ++i) s += v[i];
#pragma unroll
  for (int o = 32; o; o >>= 1) s += __shfl_xor(s, o);
  const float mean = s * (1.0f / D_);
  float q = 0.f;
#pragma unroll
  for (int i = 0; i < 16; ++i) { float d = v[i] - mean; q += d * d; }
#pragma unroll
  for (int o = 32; o; o >>= 1) q += __shfl_xor(q, o);
  const float mb = rbf(mean), vb = rbf(q * (1.0f / D_));
  const float den = rbf(sqrtf(rbf(vb + 1e-5f)));
  u32x4 oa, ob;
#pragma unroll
  for (int i = 0; i < 4; ++i) {
    oa[i] = pack2(rbf(v[2 * i] - mb) / den, rbf(v[2 * i + 1] - mb) / den);
    ob[i] = pack2(rbf(v[8 + 2 * i] - mb) / den, rbf(v[8 + 2 * i + 1] - mb) / den);
  }
  u32x4* o4 = (u32x4*)(xn + (size_t)row * D_);
  o4[lane] = oa; o4[lane + 64] = ob;
}

constexpr int G1_STAGE = 32768 + 40960;
__global__ __launch_bounds__(512, 2) void k_gemm1(const u16* __restrict__ xn, const u16* __restrict__ wqkv,
                                                  const u16* __restrict__ wg, const u16* __restrict__ wu,
                                                  u16* __restrict__ qkv, u16* __restrict__ hbuf) {
  __shared__ __attribute__((aligned(1024))) char lds[2 * G1_STAGE];
  const int tid = threadIdx.x, lane = tid & 63, wid = tid >> 6;
  const int wr = wid >> 1, wc = wid & 1;
  const int xcd = blockIdx.x & 7, idx = blockIdx.x >> 3;
  const int bm = idx & 7, bn = xcd * 4 + (idx >> 3);

  const char* pa = (const char*)xn + ((size_t)(bm * 256 + (tid >> 3)) * D_ + (((tid & 7) ^ ((tid >> 3) & 7)) * 8)) * 2;
  const char* pb[5]; int incb[5];
#pragma unroll
  for (int j = 0; j < 5; ++j) {
    const int b = j * 512 + tid, st = b >> 4, kb = st / 20, p = st % 20;
    const int r = ((b & 15) >> 1) ^ ((kb & 1) << 2), hc = b & 1, k = kb * 8 + r;
    int pw = p >= 10, s = p - pw * 10, ngu = pw ? 6 : 8;
    const u16* base; int ld, col;
    if (s < ngu) { int fi = pw * 4 + (s >> 1); base = (s & 1) ? wu : wg; ld = F_; col = bn * 112 + fi * 16; }
    else { int qi = pw ? (s - 6 + 2) : (s - 8); base = wqkv; ld = 3 * D_; col = bn * 96 + qi * 16; }
    pb[j] = (const char*)base + ((size_t)k * ld + col + hc * 8) * 2;
    incb[j] = 64 * ld * 2;
  }
  const int fr = lane & 15, g = lane >> 4;
  const int a_off0 = (wr * 64 + fr) * 128 + (((0 + g) ^ (lane & 7)) * 16);
  const int a_off1 = (wr * 64 + fr) * 128 + (((4 + g) ^ (lane & 7)) * 16);
  const int b_off0 = 32768 + (g * 20 + wc * 10) * 256 + (fr >> 2) * 32 + (lane & 3) * 8 + 128 * (0 ^ (g & 1));
  const int b_off1 = 32768 + (g * 20 + wc * 10) * 256 + (fr >> 2) * 32 + (lane & 3) * 8 + 128 * (1 ^ (g & 1));

  f32x4 acc[4][10];
#pragma unroll
  for (int m = 0; m < 4; ++m)
#pragma unroll
    for (int n = 0; n < 10; ++n) acc[m][n] = (f32x4){0.f, 0.f, 0.f, 0.f};

  auto stage = [&](int buf, bool adv) {
    char* dst = lds + buf * G1_STAGE + tid * 16;
#pragma unroll
    for (int j = 0; j < 4; ++j) glds16(pa + (size_t)j * 64 * D_ * 2, dst + j * 8192);
#pragma unroll
    for (int j = 0; j < 5; ++j) { glds16(pb[j], dst + 32768 + j * 8192); pb[j] += adv ? incb[j] : 0; }
    pa += adv ? 128 : 0;
  };

  stage(0, true);
  __syncthreads();
#pragma unroll 1
  for (int kt = 0; kt < 16; ++kt) {
    const int buf = kt & 1;
    stage(buf ^ 1, kt + 2 < 16);
    const char* base = lds + buf * G1_STAGE;
#pragma unroll
    for (int ks = 0; ks < 2; ++ks) {
      bf16x8 af[4];
#pragma unroll
      for (int m = 0; m < 4; ++m) af[m] = *(const bf16x8*)(base + (ks ? a_off1 : a_off0) + m * 2048);
#pragma unroll
      for (int n = 0; n < 10; ++n) {
        bf16x4 lo = tr_read(base + b_off0 + (ks * 80 + n) * 256);
        bf16x4 hi = tr_read(base + b_off1 + (ks * 80 + n) * 256);
        bf16x8 bfv = cat8(lo, hi);
#pragma unroll
        for (int m = 0; m < 4; ++m) acc[m][n] = __builtin_amdgcn_mfma_f32_16x16x32_bf16(bfv, af[m], acc[m][n], 0, 0, 0);
      }
    }
    __syncthreads();
  }
  __builtin_amdgcn_sched_barrier(0);
  const int row0 = bm * 256 + wr * 64 + fr;
  const int ngu = wc ? 3 : 4;
#pragma unroll
  for (int pi = 0; pi < 5; ++pi) {
    if (pi < ngu) {
      const int col = bn * 112 + (wc * 4 + pi) * 16 + g * 4;
#pragma unroll
      for (int m = 0; m < 4; ++m) {
        float hv[4];
#pragma unroll
        for (int r = 0; r < 4; ++r) {
          float gg = rbf(acc[m][2 * pi][r]), uu = rbf(acc[m][2 * pi + 1][r]);
          hv[r] = gg > 0.f ? gg * uu : 0.f;
        }
        u32x2 w = {pack2(hv[0], hv[1]), pack2(hv[2], hv[3])};
        *(u32x2*)(hbuf + (size_t)(row0 + m * 16) * F_ + col) = w;
      }
    } else {
#pragma unroll
      for (int e = 0; e < 2; ++e) {
        const int s = 2 * pi + e, qi = wc ? (s - 6 + 2) : (s - 8);
        const int col = bn * 96 + qi * 16 + g * 4;
#pragma unroll
        for (int m = 0; m < 4; ++m) {
          f32x4 a = acc[m][s];
          u32x2 w = {pack2(a[0], a[1]), pack2(a[2], a[3])};
          *(u32x2*)(qkv + (size_t)(row0 + m * 16) * (3 * D_) + col) = w;
        }
      }
    }
  }
}

__global__ __launch_bounds__(256) void k_attn(const u16* __restrict__ qkv, u16* __restrict__ o) {
  __shared__ __attribute__((aligned(16))) char ks_[320 * 64];
  __shared__ __attribute__((aligned(16))) char vs_[320 * 64];
  const int tid = threadIdx.x, lane = tid & 63, w = tid >> 6;
  const int hh = blockIdx.x & 31, q0 = (blockIdx.x >> 5) * 64;
#pragma unroll
  for (int j = 0; j < 5; ++j) {
    const int c = j * 256 + tid, i = c >> 2, ch = c & 3, key = q0 - 256 + i;
    u32x4 kv = {0, 0, 0, 0}, vv = {0, 0, 0, 0};
    if (key >= 0) {
      const u16* p = qkv + (size_t)key * (3 * D_) + hh * DH + ch * 8;
      kv = *(const u32x4*)(p + D_); vv = *(const u32x4*)(p + 2 * D_);
    }
    *(u32x4*)(ks_ + i * 64 + ch * 16) = kv; *(u32x4*)(vs_ + i * 64 + ch * 16) = vv;
  }
  const int c = lane & 15, g = lane >> 4;
  const bf16x8 qf = *(const bf16x8*)(qkv + (size_t)(q0 + 16 * w + c) * (3 * D_) + hh * DH + g * 8);
  __syncthreads();
  float sc[17][4];
  float mx = -3.0e38f;
#pragma unroll
  for (int t = 0; t < 17; ++t) {
    const bf16x8 kf = *(const bf16x8*)(ks_ + ((w + t) * 16 + c) * 64 + g * 16);
    f32x4 s = __builtin_amdgcn_mfma_f32_16x16x32_bf16(kf, qf, (f32x4){0.f, 0.f, 0.f, 0.f}, 0, 0, 0);
#pragma unroll
    for (int r = 0; r < 4; ++r) {
      const int kk = g * 4 + r, key = q0 - 256 + (w + t) * 16 + kk;
      bool valid = key >= 0;
      if (t == 0) valid = valid && (c < kk);
      if (t == 16) valid = valid && (kk <= c);
      float v = valid ? rbf(rbf(s[r]) * 0.17677669529663687f) : -1e9f;
      sc[t][r] = v; mx = fmaxf(mx, v);
    }
  }
  mx = fmaxf(mx, __shfl_xor(mx, 16)); mx = fmaxf(mx, __shfl_xor(mx, 32));
  float sum = 0.f;
#pragma unroll
  for (int t = 0; t < 17; ++t)
#pragma unroll
    for (int r = 0; r < 4; ++r) { float e = __expf(sc[t][r] - mx); sc[t][r] = e; sum += e; }
  sum += __shfl_xor(sum, 16); sum += __shfl_xor(sum, 32);
  const float inv = 1.0f / sum;
  f32x4 oacc[2] = {(f32x4){0.f, 0.f, 0.f, 0.f}, (f32x4){0.f, 0.f, 0.f, 0.f}};
#pragma unroll
  for (int u = 0; u < 9; ++u) {
    const int t0 = 2 * u, t1 = (2 * u + 1 < 17) ? 2 * u + 1 : 16;
    bf16x8 pf;
#pragma unroll
    for (int r = 0; r < 4; ++r) {
      pf[r] = (short)f2bf(sc[t0][r] * inv);
      pf[4 + r] = (2 * u + 1 < 17) ? (short)f2bf(sc[t1][r] * inv) : (short)0;
    }
#pragma unroll
    for (int dh = 0; dh < 2; ++dh) {
      bf16x4 lo = tr_read(vs_ + ((w + t0) * 16 + g * 4 + (c >> 2)) * 64 + dh * 32 + (lane & 3) * 8);
      bf16x4 hi = tr_read(vs_ + ((w + t1) * 16 + g * 4 + (c >> 2)) * 64 + dh * 32 + (lane & 3) * 8);
      oacc[dh] = __builtin_amdgcn_mfma_f32_16x16x32_bf16(cat8(lo, hi), pf, oacc[dh], 0, 0, 0);
    }
  }
#pragma unroll
  for (int dh = 0; dh < 2; ++dh) {
    u32x2 wv = {pack2(oacc[dh][0], oacc[dh][1]), pack2(oacc[dh][2], oacc[dh][3])};
    *(u32x2*)(o + (size_t)(q0 + 16 * w + c) * D_ + hh * DH + dh * 16 + g * 4) = wv;
  }
}

constexpr int G2_STAGE = 16384 + 16384;
__global__ __launch_bounds__(512, 2) void k_gemm2(const u16* __restrict__ o, const u16* __restrict__ hbuf,
                                                  const u16* __restrict__ wo, const u16* __restrict__ wd,
                                                  const u16* __restrict__ x, u16* __restrict__ out,
                                                  float* __restrict__ part, int* __restrict__ cnt) {
  __shared__ __attribute__((aligned(1024))) char lds[2 * G2_STAGE];
  const int tid = threadIdx.x, lane = tid & 63, wid = tid >> 6;
  const int kg = wid >> 2, wr = (wid >> 1) & 1, wc = wid & 1;
  const int xcd = blockIdx.x & 7, i = blockIdx.x >> 3, half = i & 1, tile = xcd * 16 + (i >> 1);
  const int tm = tile >> 3, tn = tile & 7;
  const int kt0 = half * 36;
  const int arow = tm * 128 + (tid >> 3), ac = ((tid & 7) ^ ((tid >> 3) & 7)) * 8;
  int bk[2], bc[2];
#pragma unroll
  for (int j = 0; j < 2; ++j) {
    const int b = j * 512 + tid, st = b >> 4, kb = st >> 3, nf = st & 7;
    bk[j] = kb * 8 + (((b & 15) >> 1) ^ ((kb & 1) << 2)); bc[j] = tn * 128 + nf * 16 + (b & 1) * 8;
  }
  const int fr = lane & 15, g = lane >> 4;
  const int a_off = (wr * 64 + fr) * 128 + (((kg * 4 + g) ^ (lane & 7)) * 16);
  const int b_off0 = 16384 + ((kg * 4 + g) * 8 + wc * 4) * 256 + (fr >> 2) * 32 + (lane & 3) * 8 + 128 * (0 ^ (g & 1));
  const int b_off1 = 16384 + ((kg * 4 + g) * 8 + wc * 4) * 256 + (fr >> 2) * 32 + (lane & 3) * 8 + 128 * (1 ^ (g & 1));
  f32x4 acc[4][4];
#pragma unroll
  for (int m = 0; m < 4; ++m)
#pragma unroll
    for (int n = 0; n < 4; ++n) acc[m][n] = (f32x4){0.f, 0.f, 0.f, 0.f};

  auto stage = [&](int kt, int buf) {
    char* dst = lds + buf * G2_STAGE + tid * 16;
    const u16* asrc; const u16* bsrc; int lda;
    if (kt < 16) { asrc = o + kt * 64; lda = D_; bsrc = wo + (size_t)kt * 64 * D_; }
    else { asrc = hbuf + (kt - 16) * 64; lda = F_; bsrc = wd + (size_t)(kt - 16) * 64 * D_; }
#pragma unroll
    for (int j = 0; j < 2; ++j) glds16(asrc + (size_t)(arow + j * 64) * lda + ac, dst + j * 8192);
#pragma unroll
    for (int j = 0; j < 2; ++j) glds16(bsrc + (size_t)bk[j] * D_ + bc[j], dst + 16384 + j * 8192);
  };
  stage(kt0, 0);
  __syncthreads();
  for (int it = 0; it < 36; ++it) {
    const int buf = it & 1;
    if (it + 1 < 36) stage(kt0 + it + 1, buf ^ 1);
    const char* base = lds + buf * G2_STAGE;
    bf16x8 af[4];
#pragma unroll
    for (int m = 0; m < 4; ++m) af[m] = *(const bf16x8*)(base + a_off + m * 2048);
#pragma unroll
    for (int n = 0; n < 4; ++n) {
      bf16x8 bfv = cat8(tr_read(base + b_off0 + n * 256), tr_read(base + b_off1 + n * 256));
#pragma unroll
      for (int m = 0; m < 4; ++m) acc[m][n] = __builtin_amdgcn_mfma_f32_16x16x32_bf16(bfv, af[m], acc[m][n], 0, 0, 0);
    }
    __syncthreads();
  }
  f32x4* red = (f32x4*)lds + (size_t)(wid & 3) * 16 * 64;
  if (kg == 1) {
#pragma unroll
    for (int m = 0; m < 4; ++m)
#pragma unroll
      for (int n = 0; n < 4; ++n) red[(m * 4 + n) * 64 + lane] = acc[m][n];
  }
  __syncthreads();
  __shared__ int s_last;
  if (kg == 0) {
#pragma unroll
    for (int m = 0; m < 4; ++m)
#pragma unroll
      for (int n = 0; n < 4; ++n) acc[m][n] += red[(m * 4 + n) * 64 + lane];
    f32x4* dstp = (f32x4*)part + ((size_t)(half * 128 + tile) * 4 + (wid & 3)) * 1024;
#pragma unroll
    for (int m = 0; m < 4; ++m)
#pragma unroll
      for (int n = 0; n < 4; ++n) dstp[(m * 4 + n) * 64 + lane] = acc[m][n];
  }
  asm volatile("s_waitcnt vmcnt(0)" ::: "memory");
  __syncthreads();
  if (tid == 0) {
    __builtin_amdgcn_fence(__ATOMIC_RELEASE, "agent");
    asm volatile("s_waitcnt vmcnt(0)" ::: "memory");
    int t = __hip_atomic_fetch_add(&cnt[tile], 1, __ATOMIC_RELAXED, __HIP_MEMORY_SCOPE_AGENT);
    if (t == 1) { __builtin_amdgcn_fence(__ATOMIC_ACQUIRE, "agent"); asm volatile("s_waitcnt vmcnt(0)" ::: "memory"); }
    s_last = t;
  }
  __syncthreads();
  if (s_last != 1 || kg != 0) return;
  __builtin_amdgcn_fence(__ATOMIC_ACQUIRE, "agent");
  const f32x4* srcp = (const f32x4*)part + ((size_t)((half ^ 1) * 128 + tile) * 4 + (wid & 3)) * 1024;
#pragma unroll
  for (int m = 0; m < 4; ++m) {
    const size_t rowoff = (size_t)(tm * 128 + wr * 64 + m * 16 + fr) * D_ + tn * 128 + wc * 64 + g * 4;
#pragma unroll
    for (int n = 0; n < 4; ++n) {
      f32x4 a = acc[m][n] + __builtin_nontemporal_load(&srcp[(m * 4 + n) * 64 + lane]);
      u32x2 xv = *(const u32x2*)(x + rowoff + n * 16);
      float r0 = a[0] + __uint_as_float(xv[0] << 16), r1 = a[1] + __uint_as_float(xv[0] & 0xffff0000u);
      float r2 = a[2] + __uint_as_float(xv[1] << 16), r3 = a[3] + __uint_as_float(xv[1] & 0xffff0000u);
      u32x2 wv = {pack2(r0, r1), pack2(r2, r3)};
      *(u32x2*)(out + rowoff + n * 16) = wv;
    }
  }
}

extern "C" void kernel_launch(void* const* d_in, const int* in_sizes, int n_in, void* d_out, int out_size, void* d_ws,
                              size_t ws_size, hipStream_t stream) {
  const u16* x = (const u16*)d_in[0];
  const u16* wqkv = (const u16*)d_in[1];
  const u16* wo = (const u16*)d_in[2];
  const u16* wg = (const u16*)d_in[4];
  const u16* wu = (const u16*)d_in[5];
  const u16* wd = (const u16*)d_in[6];
  char* ws = (char*)d_ws;
  u16* xn = (u16*)ws;
  u16* qkv = (u16*)(ws + (4u << 20));
  u16* hbuf = (u16*)(ws + (16u << 20));
  u16* o = (u16*)(ws + (32u << 20));
  float* part = (float*)(ws + (40u << 20));
  int* cnt = (int*)(ws + (60u << 20));
  k_ln<<<S_ / 4, 256, 0, stream>>>(x, xn, cnt);
  k_gemm1<<<256, 512, 0, stream>>>(xn, wqkv, wg, wu, qkv, hbuf);
  k_attn<<<H_ * (S_ / 64), 256, 0, stream>>>(qkv, o);
  k_gemm2<<<256, 512, 0, stream>>>(o, hbuf, wo, wd, x, (u16*)d_out, part, cnt);
}
```

```cpp
#include <hip/hip_runtime.h>
#include <hip/hip_bf16.h>

typedef __attribute__((ext_vector_type(8))) short bf16x8;
typedef __attribute__((ext_vector_type(4))) short bf16x4;
typedef __attribute__((ext_vector_type(4))) float f32x4;
typedef __attribute__((ext_vector_type(4))) unsigned u32x4;
typedef __attribute__((ext_vector_type(2))) unsigned u32x2;
typedef unsigned short u16;
#define LDS_AS __attribute__((address_space(3)))

constexpr int S_ = 2048, D_ = 1024, F_ = 3584, H_ = 32, DH = 32, WIN = 256;

__device__ __forceinline__ float bf2f(u16 v) { return __uint_as_float(((unsigned)v) << 16); }
__device__ __forceinline__ u16 f2bf(float f) {
  unsigned u = __float_as_uint(f);
  u += 0x7fffu + ((u >> 16) & 1u);
  return (u16)(u >> 16);
}
__device__ __forceinline__ float rbf(float f) { return bf2f(f2bf(f)); }
__device__ __forceinline__ unsigned pack2(float a, float b) {
  unsigned r; asm("v_cvt_pk_bf16_f32 %0, %1, %2" : "=v"(r) : "v"(a), "v"(b)); return r;
}

__device__ __forceinline__ void glds16(const void* g, unsigned lds_addr) {
  unsigned keep;
  asm volatile("s_mov_b32 %0, m0\n\ts_mov_b32 m0, %2\n\ts_nop 0\n\tglobal_load_lds_dwordx4 %1, off\n\ts_mov_b32 m0, %0"
               : "=&s"(keep) : "v"(g), "s"(lds_addr) : "memory");
}
__device__ __forceinline__ unsigned lds_u32(const void* p) { return (unsigned)(size_t)(LDS_AS const char*)p; }
__device__ __forceinline__ bf16x4 tr_read(const void* l) {
  return __builtin_amdgcn_ds_read_tr16_b64_v4i16((LDS_AS bf16x4*)l);
}
__device__ __forceinline__ bf16x8 cat8(bf16x4 a, bf16x4 b) {
  return __builtin_shufflevector(a, b, 0, 1, 2, 3, 4, 5, 6, 7);
}

__global__ __launch_bounds__(256) void k_ln(const u16* __restrict__ x, u16* __restrict__ xn) {
  const int row = blockIdx.x * 4 + (threadIdx.x >> 6), lane = threadIdx.x & 63;
  const u32x4* p = (const u32x4*)(x + (size_t)row * D_);
  u32x4 a = p[lane], b = p[lane + 64];
  float v[16];
#pragma unroll
  for (int i = 0; i < 4; ++i) {
    v[2 * i] = __uint_as_float(a[i] << 16); v[2 * i + 1] = __uint_as_float(a[i] & 0xffff0000u);
    v[8 + 2 * i] = __uint_as_float(b[i] << 16); v[8 + 2 * i + 1] = __uint_as_float(b[i] & 0xffff0000u);
  }
  float s = 0.f;
#pragma unroll
  for (int i = 0; i < 16; ++i) s += v[i];
#pragma unroll
  for (int o = 32; o; o >>= 1) s += __shfl_xor(s, o);
  const float mean = s * (1.0f / D_);
  float q = 0.f;
#pragma unroll
  for (int i = 0; i < 16; ++i) { float d = v[i] - mean; q += d * d; }
#pragma unroll
  for (int o = 32; o; o >>= 1) q += __shfl_xor(q, o);
  const float mb = rbf(mean), vb = rbf(q * (1.0f / D_));
  const float den = rbf(sqrtf(rbf(vb + 1e-5f)));
  u32x4 oa, ob;
#pragma unroll
  for (int i = 0; i < 4; ++i) {
    oa[i] = pack2(rbf(v[2 * i] - mb) / den, rbf(v[2 * i + 1] - mb) / den);
    ob[i] = pack2(rbf(v[8 + 2 * i] - mb) / den, rbf(v[8 + 2 * i + 1] - mb) / den);
  }
  u32x4* o4 = (u32x4*)(xn + (size_t)row * D_);
  o4[lane] = oa; o4[lane + 64] = ob;
}

constexpr int G1_STAGE = 32768 + 40960;
__global__ __launch_bounds__(512, 2) void k_gemm1(const u16* __restrict__ xn, const u16* __restrict__ wqkv,
                                                  const u16* __restrict__ wg, const u16* __restrict__ wu,
                                                  u16* __restrict__ qkv, u16* __restrict__ hbuf) {
  __shared__ __attribute__((aligned(1024))) char lds[2 * G1_STAGE];
  const int tid = threadIdx.x, lane = tid & 63, wid = tid >> 6;
  const int wr = wid >> 1, wc = wid & 1;
  const int xcd = blockIdx.x & 7, idx = blockIdx.x >> 3;
  const int bm = idx & 7, bn = xcd * 4 + (idx >> 3);

  const char* pa = (const char*)xn + ((size_t)(bm * 256 + (tid >> 3)) * D_ + (((tid & 7) ^ ((tid >> 3) & 7)) * 8)) * 2;
  const char* pb[5]; int incb[5];
#pragma unroll
  for (int j = 0; j < 5; ++j) {
    const int b = j * 512 + tid, st = b >> 4, kb = st / 20, p = st % 20;
    const int r = ((b & 15) >> 1) ^ ((kb & 1) << 2), hc = b & 1, k = kb * 8 + r;
    const int grp = p >> 2, e = p & 3;
    const u16* base; int ld, col;
    if (grp == 2 || e == 3 && grp >= 3) { int qi = grp == 2 ? e : grp + 1; base = wqkv; ld = 3 * D_; col = bn * 96 + qi * 16; }
    else { int fi = (grp >= 3 ? 4 : 0) + e; base = (grp == 1 || grp == 4) ? wu : wg; ld = F_; col = bn * 112 + fi * 16; }
    pb[j] = (const char*)base + ((size_t)k * ld + col + hc * 8) * 2;
    incb[j] = 64 * ld * 2;
  }
  const unsigned ldsw = lds_u32(lds) + __builtin_amdgcn_readfirstlane(wid) * 1024;
  const int fr = lane & 15, g = lane >> 4;
  const int a_off0 = (wr * 64 + fr) * 128 + (((0 + g) ^ (lane & 7)) * 16);
  const int a_off1 = (wr * 64 + fr) * 128 + (((4 + g) ^ (lane & 7)) * 16);
  const int b_off0 = 32768 + (g * 20 + wc * 10) * 256 + (fr >> 2) * 32 + (lane & 3) * 8 + 128 * (0 ^ (g & 1));
  const int b_off1 = 32768 + (g * 20 + wc * 10) * 256 + (fr >> 2) * 32 + (lane & 3) * 8 + 128 * (1 ^ (g & 1));

  f32x4 acc[4][10];
#pragma unroll
  for (int m = 0; m < 4; ++m)
#pragma unroll
    for (int n = 0; n < 10; ++n) acc[m][n] = (f32x4){0.f, 0.f, 0.f, 0.f};

  auto stage = [&](int buf, bool adv) {
    const unsigned dst = ldsw + buf * G1_STAGE;
#pragma unroll
    for (int j = 0; j < 4; ++j) glds16(pa + (size_t)j * 64 * D_ * 2, dst + j * 8192);
#pragma unroll
    for (int j = 0; j < 5; ++j) { glds16(pb[j], dst + 32768 + j * 8192); pb[j] += adv ? incb[j] : 0; }
    pa += adv ? 128 : 0;
  };

  stage(0, true);
  asm volatile("s_waitcnt vmcnt(0)" ::: "memory");
  __syncthreads();
#pragma unroll 1
  for (int kt = 0; kt < 16; ++kt) {
    const int buf = kt & 1;
    stage(buf ^ 1, kt + 2 < 16);
    const char* base = lds + buf * G1_STAGE;
#pragma unroll
    for (int ks = 0; ks < 2; ++ks) {
      bf16x8 af[4];
#pragma unroll
      for (int m = 0; m < 4; ++m) af[m] = *(const bf16x8*)(base + (ks ? a_off1 : a_off0) + m * 2048);
#pragma unroll
      for (int n = 0; n < 10; ++n) {
        bf16x4 lo = tr_read(base + b_off0 + (ks * 80 + n) * 256);
        bf16x4 hi = tr_read(base + b_off1 + (ks * 80 + n) * 256);
        bf16x8 bfv = cat8(lo, hi);
#pragma unroll
        for (int m = 0; m < 4; ++m) acc[m][n] = __builtin_amdgcn_mfma_f32_16x16x32_bf16(bfv, af[m], acc[m][n], 0, 0, 0);
      }
    }
    asm volatile("s_waitcnt vmcnt(0)" ::: "memory");
    __syncthreads();
  }
  __builtin_amdgcn_sched_barrier(0);
  char* himg = lds; char* qimg = lds + 57344;
  const int lrow = wr * 64 + fr;
  auto st_h = [&](int sg, int su, int fi) {
#pragma unroll
    for (int m = 0; m < 4; ++m) {
      float hv[4];
#pragma unroll
      for (int r = 0; r < 4; ++r) {
        float gg = rbf(acc[m][sg][r]), uu = rbf(acc[m][su][r]);
        hv[r] = gg > 0.f ? gg * uu : 0.f;
      }
      u32x2 w = {pack2(hv[0], hv[1]), pack2(hv[2], hv[3])};
      *(u32x2*)(himg + (lrow + m * 16) * 224 + fi * 32 + g * 8) = w;
    }
  };
  auto st_q = [&](int sq, int qi) {
#pragma unroll
    for (int m = 0; m < 4; ++m) {
      f32x4 a = acc[m][sq];
      u32x2 w = {pack2(a[0], a[1]), pack2(a[2], a[3])};
      *(u32x2*)(qimg + (lrow + m * 16) * 192 + qi * 32 + g * 8) = w;
    }
  };
  if (wc == 0) {
    st_h(0, 4, 0); st_h(1, 5, 1); st_h(2, 6, 2); st_h(3, 7, 3); st_q(8, 0); st_q(9, 1);
  } else {
    st_q(0, 2); st_q(1, 3); st_h(2, 6, 4); st_h(3, 7, 5); st_h(4, 8, 6); st_q(5, 4); st_q(9, 5);
  }
  __syncthreads();
#pragma unroll
  for (int j = 0; j < 7; ++j) {
    const int c = j * 512 + tid, r = c / 14, ch = c - r * 14;
    *(u32x4*)(hbuf + (size_t)(bm * 256 + r) * F_ + bn * 112 + ch * 8) = *(const u32x4*)(himg + r * 224 + ch * 16);
  }
#pragma unroll
  for (int j = 0; j < 6; ++j) {
    const int c = j * 512 + tid, r = c / 12, ch = c - r * 12;
    *(u32x4*)(qkv + (size_t)(bm * 256 + r) * (3 * D_) + bn * 96 + ch * 8) = *(const u32x4*)(qimg + r * 192 + ch * 16);
  }
}

__global__ __launch_bounds__(512) void k_attn(const u16* __restrict__ qkv, u16* __restrict__ o) {
  __shared__ __attribute__((aligned(16))) char kv_[4 * 320 * 64];
  const int tid = threadIdx.x, lane = tid & 63, w = (tid >> 6) & 3, hs = tid >> 8;
  const int hp = blockIdx.x & 15, q0 = (blockIdx.x >> 4) * 64, hh = hp * 2 + hs;
#pragma unroll
  for (int j = 0; j < 5; ++j) {
    const int c = j * 512 + tid, i = c >> 3, ch = c & 7, key = q0 - 256 + i;
    u32x4 kv = {0, 0, 0, 0}, vv = {0, 0, 0, 0};
    if (key >= 0) {
      const u16* p = qkv + (size_t)key * (3 * D_) + hp * 64 + ch * 8;
      kv = *(const u32x4*)(p + D_); vv = *(const u32x4*)(p + 2 * D_);
    }
    char* dst = kv_ + (ch >> 2) * 40960 + i * 64 + (ch & 3) * 16;
    *(u32x4*)dst = kv; *(u32x4*)(dst + 20480) = vv;
  }
  const char* ks_ = kv_ + hs * 40960; const char* vs_ = ks_ + 20480;
  const int c = lane & 15, g = lane >> 4;
  const bf16x8 qf = *(const bf16x8*)(qkv + (size_t)(q0 + 16 * w + c) * (3 * D_) + hh * DH + g * 8);
  __syncthreads();
  float sc[17][4];
  float mx = -3.0e38f;
  const float kscale = 0.17677669529663687f * 1.4426950408889634f;
#pragma unroll
  for (int t = 0; t < 17; ++t) {
    const bf16x8 kf = *(const bf16x8*)(ks_ + ((w + t) * 16 + c) * 64 + g * 16);
    f32x4 s = __builtin_amdgcn_mfma_f32_16x16x32_bf16(kf, qf, (f32x4){0.f, 0.f, 0.f, 0.f}, 0, 0, 0);
    const bool tile_ok = q0 - 256 + (w + t) * 16 >= 0;
#pragma unroll
    for (int r = 0; r < 4; ++r) {
      const int kk = g * 4 + r;
      bool valid = tile_ok;
      if (t == 0) valid = valid && (c < kk);
      if (t == 16) valid = valid && (kk <= c);
      float v = valid ? s[r] * kscale : -1e30f;
      sc[t][r] = v; mx = fmaxf(mx, v);
    }
  }
  mx = fmaxf(mx, __shfl_xor(mx, 16)); mx = fmaxf(mx, __shfl_xor(mx, 32));
  float sum = 0.f;
#pragma unroll
  for (int t = 0; t < 17; ++t)
#pragma unroll
    for (int r = 0; r < 4; ++r) { float e = __builtin_amdgcn_exp2f(sc[t][r] - mx); sc[t][r] = e; sum += e; }
  sum += __shfl_xor(sum, 16); sum += __shfl_xor(sum, 32);
  const float inv = 1.0f / sum;
  f32x4 oacc[2] = {(f32x4){0.f, 0.f, 0.f, 0.f}, (f32x4){0.f, 0.f, 0.f, 0.f}};
#pragma unroll
  for (int u = 0; u < 9; ++u) {
    const int t0 = 2 * u, t1 = (2 * u + 1 < 17) ? 2 * u + 1 : 16;
    u32x4 pw;
    pw[0] = pack2(sc[t0][0], sc[t0][1]); pw[1] = pack2(sc[t0][2], sc[t0][3]);
    if (2 * u + 1 < 17) { pw[2] = pack2(sc[t1][0], sc[t1][1]); pw[3] = pack2(sc[t1][2], sc[t1][3]); }
    else { pw[2] = 0; pw[3] = 0; }
    const bf16x8 pf = __builtin_bit_cast(bf16x8, pw);
#pragma unroll
    for (int dh = 0; dh < 2; ++dh) {
      bf16x4 lo = tr_read(vs_ + ((w + t0) * 16 + g * 4 + (c >> 2)) * 64 + dh * 32 + (lane & 3) * 8);
      bf16x4 hi = tr_read(vs_ + ((w + t1) * 16 + g * 4 + (c >> 2)) * 64 + dh * 32 + (lane & 3) * 8);
      oacc[dh] = __builtin_amdgcn_mfma_f32_16x16x32_bf16(cat8(lo, hi), pf, oacc[dh], 0, 0, 0);
    }
  }
  oacc[0] *= inv; oacc[1] *= inv;
#pragma unroll
  for (int dh = 0; dh < 2; ++dh) {
    u32x2 wv = {pack2(oacc[dh][0], oacc[dh][1]), pack2(oacc[dh][2], oacc[dh][3])};
    *(u32x2*)(o + (size_t)(q0 + 16 * w + c) * D_ + hh * DH + dh * 16 + g * 4) = wv;
  }
}

constexpr int G2_STAGE = 16384 + 16384;
__global__ __launch_bounds__(512, 2) void k_gemm2(const u16* __restrict__ o, const u16* __restrict__ hbuf,
                                                  const u16* __restrict__ wo, const u16* __restrict__ wd,
                                                  float* __restrict__ part) {
  __shared__ __attribute__((aligned(1024))) char lds[4 * G2_STAGE];
  const int tid = threadIdx.x, lane = tid & 63, wid = tid >> 6;
  const int kg = wid >> 2, wr = (wid >> 1) & 1, wc = wid & 1;
  const int xcd = blockIdx.x & 7, i = blockIdx.x >> 3, half = i & 1, tile = xcd * 16 + (i >> 1);
  const int tm = tile >> 3, tn = tile & 7;
  const int kt0 = half * 36;
  const int arow = tm * 128 + (tid >> 3), ac = ((tid & 7) ^ ((tid >> 3) & 7)) * 8;
  int bk[2], bc[2];
#pragma unroll
  for (int j = 0; j < 2; ++j) {
    const int b = j * 512 + tid, st = b >> 4, kb = st >> 3, nf = st & 7;
    bk[j] = kb * 8 + (((b & 15) >> 1) ^ ((kb & 1) << 2)); bc[j] = tn * 128 + nf * 16 + (b & 1) * 8;
  }
  const int fr = lane & 15, g = lane >> 4;
  const int a_off = (wr * 64 + fr) * 128 + (((kg * 4 + g) ^ (lane & 7)) * 16);
  const int b_off0 = 16384 + ((kg * 4 + g) * 8 + wc * 4) * 256 + (fr >> 2) * 32 + (lane & 3) * 8 + 128 * (0 ^ (g & 1));
  const int b_off1 = 16384 + ((kg * 4 + g) * 8 + wc * 4) * 256 + (fr >> 2) * 32 + (lane & 3) * 8 + 128 * (1 ^ (g & 1));
  f32x4 acc[4][4];
#pragma unroll
  for (int m = 0; m < 4; ++m)
#pragma unroll
    for (int n = 0; n < 4; ++n) acc[m][n] = (f32x4){0.f, 0.f, 0.f, 0.f};

  auto stage = [&](int kt, int buf) {
    const unsigned dst = lds_u32(lds) + __builtin_amdgcn_readfirstlane(wid) * 1024 + buf * G2_STAGE;
    const u16* asrc; const u16* bsrc; int lda;
    if (kt < 16) { asrc = o + kt * 64; lda = D_; bsrc = wo + (size_t)kt * 64 * D_; }
    else { asrc = hbuf + (kt - 16) * 64; lda = F_; bsrc = wd + (size_t)(kt - 16) * 64 * D_; }
#pragma unroll
    for (int j = 0; j < 2; ++j) glds16(asrc + (size_t)(arow + j * 64) * lda + ac, dst + j * 8192);
#pragma unroll
    for (int j = 0; j < 2; ++j) glds16(bsrc + (size_t)bk[j] * D_ + bc[j], dst + 16384 + j * 8192);
  };
  stage(kt0, 0); stage(kt0 + 1, 1); stage(kt0 + 2, 2);
#pragma unroll 1
  for (int it = 0; it < 36; ++it) {
    const int buf = it & 3;
    asm volatile("s_waitcnt vmcnt(8)" ::: "memory");
    __builtin_amdgcn_s_barrier();
    asm volatile("" ::: "memory");
    stage(kt0 + (it + 3 < 36 ? it + 3 : 35), (it + 3) & 3);
    const char* base = lds + buf * G2_STAGE;
    bf16x8 af[4];
#pragma unroll
    for (int m = 0; m < 4; ++m) af[m] = *(const bf16x8*)(base + a_off + m * 2048);
#pragma unroll
    for (int n = 0; n < 4; ++n) {
      bf16x8 bfv = cat8(tr_read(base + b_off0 + n * 256), tr_read(base + b_off1 + n * 256));
#pragma unroll
      for (int m = 0; m < 4; ++m) acc[m][n] = __builtin_amdgcn_mfma_f32_16x16x32_bf16(bfv, af[m], acc[m][n], 0, 0, 0);
    }
  }
  asm volatile("s_waitcnt vmcnt(0)" ::: "memory");
  __syncthreads();
  f32x4* red = (f32x4*)lds + (size_t)(wid & 3) * 16 * 64;
  if (kg == 1) {
#pragma unroll
    for (int m = 0; m < 4; ++m)
#pragma unroll
      for (int n = 0; n < 4; ++n) red[(m * 4 + n) * 64 + lane] = acc[m][n];
  }
  __syncthreads();
  if (kg == 0) {
    float* dstp = part + (size_t)half * S_ * D_;
#pragma unroll
    for (int m = 0; m < 4; ++m) {
      const size_t rowoff = (size_t)(tm * 128 + wr * 64 + m * 16 + fr) * D_ + tn * 128 + wc * 64 + g * 4;
#pragma unroll
      for (int n = 0; n < 4; ++n) *(f32x4*)(dstp + rowoff + n * 16) = acc[m][n] + red[(m * 4 + n) * 64 + lane];
    }
  }
}

__global__ __launch_bounds__(256) void k_reduce(const u16* __restrict__ x, const float* __restrict__ part, u16* __restrict__ out) {
  const size_t i = ((size_t)blockIdx.x * 256 + threadIdx.x) * 8;
  const f32x4 a0 = *(const f32x4*)(part + i), a1 = *(const f32x4*)(part + i + 4);
  const f32x4 b0 = *(const f32x4*)(part + (size_t)S_ * D_ + i), b1 = *(const f32x4*)(part + (size_t)S_ * D_ + i + 4);
  const u32x4 xv = *(const u32x4*)(x + i);
  f32x4 s0 = a0 + b0, s1 = a1 + b1;
  u32x4 w;
  w[0] = pack2(s0[0] + __uint_as_float(xv[0] << 16), s0[1] + __uint_as_float(xv[0] & 0xffff0000u));
  w[1] = pack2(s0[2] + __uint_as_float(xv[1] << 16), s0[3] + __uint_as_float(xv[1] & 0xffff0000u));
  w[2] = pack2(s1[0] + __uint_as_float(xv[2] << 16), s1[1] + __uint_as_float(xv[2] & 0xffff0000u));
  w[3] = pack2(s1[2] + __uint_as_float(xv[3] << 16), s1[3] + __uint_as_float(xv[3] & 0xffff0000u));
  *(u32x4*)(out + i) = w;
}

extern "C" void kernel_launch(void* const* d_in, const int* in_sizes, int n_in, void* d_out, int out_size, void* d_ws,
                              size_t ws_size, hipStream_t stream) {
  const u16* x = (const u16*)d_in[0];
  const u16* wqkv = (const u16*)d_in[1];
  const u16* wo = (const u16*)d_in[2];
  const u16* wg = (const u16*)d_in[4];
  const u16* wu = (const u16*)d_in[5];
  const u16* wd = (const u16*)d_in[6];
  char* ws = (char*)d_ws;
  u16* xn = (u16*)ws;
  u16* qkv = (u16*)(ws + (4u << 20));
  u16* hbuf = (u16*)(ws + (16u << 20));
  u16* o = (u16*)(ws + (32u << 20));
  float* part = (float*)(ws + (40u << 20));
  k_ln<<<S_ / 4, 256, 0, stream>>>(x, xn);
  k_gemm1<<<256, 512, 0, stream>>>(xn, wqkv, wg, wu, qkv, hbuf);
  k_attn<<<(H_ / 2) * (S_ / 64), 512, 0, stream>>>(qkv, o);
  k_gemm2<<<256, 512, 0, stream>>>(o, hbuf, wo, wd, part);
  k_reduce<<<S_ * D_ / 2048, 256, 0, stream>>>(x, part, (u16*)d_out);
}
```
